```python
import torch

def _softmax(x, axis=-1):
    return torch.softmax(x, dim=axis)

def _gelu(x):
    return torch.nn.functional.gelu(x, approximate='tanh')

def _silu(x):
    return torch.nn.functional.silu(x)

def _sigmoid(x):
    return torch.sigmoid(x)

def _layernorm(x, axis=-1, eps=1e-05):
    m = x.mean(dim=axis, keepdim=True)
    v = x.var(dim=axis, keepdim=True, unbiased=False)
    return (x - m) / torch.sqrt(v + eps)

def _scatter_add(tgt, idx, src, axis=0):
    return tgt.clone().index_add_(axis, idx, src)

def _segment_sum(src, seg, n):
    out = torch.zeros((n,) + src.shape[1:], dtype=src.dtype)
    return out.index_add_(0, seg, src)

def _pad(x, pw):
    flat = tuple((v for pair in reversed(pw) for v in pair))
    return torch.nn.functional.pad(x, flat)
kernel_name = 'standalone__autoregressive_conv_wavenet__speech'

def setup_inputs():

    _w = lambda a: (lambda t: t.to(torch.bfloat16) if t.is_floating_point() else t)(torch.as_tensor(a))
    B, T, D, K = (2, 4000, 256, 3)
    return {'x': _w((lambda *s: torch.randn(*s, dtype=torch.bfloat16) * 0.1)(B, T, D)), 'w': _w((lambda *s: torch.randn(*s, dtype=torch.bfloat16) * 0.1)(K, D, 2 * D)), 'w_o': _w((lambda *s: torch.randn(*s, dtype=torch.bfloat16) * 0.05)(D, D))}

def reference(x, w, w_o):
    B, T, D = x.shape
    K = w.shape[0]
    dil = 2
    pad = (K - 1) * dil
    xp = _pad(x, ((0, 0), (pad, 0), (0, 0)))
    h = torch.einsum('btc,cd->btd', xp[:, pad:pad + T, :], w[0])
    for i in range(1, K):
        off = pad - i * dil
        h = h + torch.einsum('btc,cd->btd', xp[:, off:off + T, :], w[i])
    f = h[..., :D].clone()
    g = h[..., D:].clone()
    z = torch.tanh(f) * _sigmoid(g)
    return z @ w_o
```

```cpp
#include <hip/hip_runtime.h>
#include <hip/hip_bf16.h>

typedef __attribute__((ext_vector_type(8))) short bf16x8;
typedef __attribute__((ext_vector_type(4))) short s16x4;
typedef __attribute__((ext_vector_type(16))) float f32x16;
typedef __attribute__((ext_vector_type(4))) unsigned u32x4;
typedef __attribute__((ext_vector_type(2))) unsigned u32x2;
#define DI __device__ __forceinline__
#define LDSP(p) ((__attribute__((address_space(3))) void*)(p))
#define MFMA(a, b, c) __builtin_amdgcn_mfma_f32_32x32x16_bf16((a), (b), (c), 0, 0, 0)

constexpr int TT = 4000, MM = 8000, DD = 256;

DI unsigned pk_bf16(float lo, float hi) {
  unsigned r;
  asm("v_cvt_pk_bf16_f32 %0, %1, %2" : "=v"(r) : "v"(lo), "v"(hi));
  return r;
}
DI s16x4 tr_rd(const char* p) {
  return __builtin_amdgcn_ds_read_tr16_b64_v4i16((__attribute__((address_space(3))) s16x4*)(p));
}
DI unsigned swzb(unsigned row) { return ((row & 3) << 2) | ((row >> 2) & 3); }

constexpr int XREG = 148 * 64;
constexpr int WREG = 3 * 8192;
constexpr int BUFB = XREG + WREG;
constexpr int NBUF = 4;

__global__ __launch_bounds__(256, 1) void k1(const unsigned short* __restrict__ x, const unsigned short* __restrict__ w,
                                             unsigned short* __restrict__ zws) {
  __shared__ __attribute__((aligned(16))) char lds[NBUF * BUFB];
  const int tid = threadIdx.x, lane = tid & 63;
  const int wave = __builtin_amdgcn_readfirstlane(tid >> 6);
  const int bid = blockIdx.x;
  const int mt = (bid & 7) + 8 * ((bid >> 3) >> 2);
  const int ns = (bid >> 3) & 3;
  if (mt >= 63) return;
  const int r0 = mt * 128, c0 = ns * 64;
  const int wm = wave >> 1, wn = wave & 1;

  if (tid < 64) {
    u32x4 zz = {0, 0, 0, 0};
    *(u32x4*)(lds + (tid >> 4) * BUFB + 144 * 64 + (tid & 15) * 16) = zz;
  }

  unsigned xsrc[3];
#pragma unroll
  for (int e = 0; e < 3; ++e) {
    int q = wave + 4 * e;
    int row = 16 * q + (lane >> 2);
    int ch = (lane & 3) ^ ((row >> 2) & 3);
    int gr = r0 - 4 + row;
    gr = gr < 0 ? 0 : (gr > MM - 1 ? MM - 1 : gr);
    xsrc[e] = (unsigned)gr * 512u + ch * 16u;
  }
  unsigned wsrc[6];
#pragma unroll
  for (int e = 0; e < 6; ++e) {
    int idx = wave * 6 + e;
    int tap = idx >> 3, q = idx & 7;
    int row = 4 * q + (lane >> 4);
    int ch = (lane & 15) ^ swzb(row);
    int col = ch < 8 ? c0 + ch * 8 : 256 + c0 + (ch - 8) * 8;
    wsrc[e] = (unsigned)((tap * 256 + row) * 512 + col) * 2u;
  }
  const char* xb = (const char*)x;
  const char* wb = (const char*)w;

  auto issue = [&](int s) {
    char* bb = lds + (s & (NBUF - 1)) * BUFB;
#pragma unroll
    for (int e = 0; e < 3; ++e) {
      if (e < 2 || wave == 0)
        __builtin_amdgcn_global_load_lds((const unsigned*)(xb + xsrc[e] + s * 64), LDSP(bb + (wave + 4 * e) * 1024), 16, 0, 0);
    }
#pragma unroll
    for (int e = 0; e < 6; ++e)
      __builtin_amdgcn_global_load_lds((const unsigned*)(wb + wsrc[e] + s * 32768), LDSP(bb + XREG + (wave * 6 + e) * 1024), 16, 0, 0);
  };

  const int m = lane & 31, h = lane >> 5;
  unsigned xo[2][3][2];
#pragma unroll
  for (int g = 0; g < 2; ++g)
#pragma unroll
    for (int i = 0; i < 3; ++i) {
      int lm = wm * 64 + g * 32 + m;
      int r = r0 + lm;
      int t = r >= TT ? r - TT : r;
      int xrow = lm + 4 - 2 * i;
      bool ok = t >= 2 * i;
#pragma unroll
      for (int ks = 0; ks < 2; ++ks) {
        int ch = 2 * ks + h;
        xo[g][i][ks] = ok ? (unsigned)(xrow * 64 + ((ch ^ ((xrow >> 2) & 3)) * 16)) : (unsigned)(144 * 64);
      }
    }
  unsigned wo[2][2][2];
  {
    const unsigned blk = (lane >> 4) & 1, q = (lane & 15) >> 2, p = lane & 3;
#pragma unroll
    for (int tl = 0; tl < 2; ++tl)
#pragma unroll
      for (int ks = 0; ks < 2; ++ks)
#pragma unroll
        for (int t = 0; t < 2; ++t) {
          unsigned row = 16 * ks + 8 * h + 4 * t + q;
          unsigned ch = 4 * (wn + 2 * tl) + 2 * blk + (p >> 1);
          wo[tl][ks][t] = XREG + 256 * row + 16 * (ch ^ swzb(row)) + 8 * (p & 1);
        }
  }

  f32x16 acc[2][2];
#pragma unroll
  for (int a = 0; a < 2; ++a)
#pragma unroll
    for (int b = 0; b < 2; ++b)
#pragma unroll
      for (int i = 0; i < 16; ++i) acc[a][b][i] = 0.f;

  issue(0);
  issue(1);
  issue(2);

#pragma unroll
  for (int s = 0; s < 8; ++s) {
    if (s < 6) asm volatile("s_waitcnt vmcnt(16)" ::: "memory");
    else if (s == 6) asm volatile("s_waitcnt vmcnt(8)" ::: "memory");
    else asm volatile("s_waitcnt vmcnt(0)" ::: "memory");
    __builtin_amdgcn_s_barrier();
    asm volatile("" ::: "memory");
    if (s + 3 < 8) issue(s + 3);
    const char* bb = lds + (s & (NBUF - 1)) * BUFB;
#pragma unroll
    for (int i = 0; i < 3; ++i)
#pragma unroll
      for (int ks = 0; ks < 2; ++ks) {
        bf16x8 af[2], bfr[2];
#pragma unroll
        for (int tl = 0; tl < 2; ++tl) {
          s16x4 lo = tr_rd(bb + i * 8192 + wo[tl][ks][0]);
          s16x4 hi = tr_rd(bb + i * 8192 + wo[tl][ks][1]);
          af[tl] = __builtin_shufflevector(lo, hi, 0, 1, 2, 3, 4, 5, 6, 7);
        }
#pragma unroll
        for (int g = 0; g < 2; ++g) bfr[g] = *(const bf16x8*)(bb + xo[g][i][ks]);
#pragma unroll
        for (int tl = 0; tl < 2; ++tl)
#pragma unroll
          for (int g = 0; g < 2; ++g) acc[tl][g] = MFMA(af[tl], bfr[g], acc[tl][g]);
      }
    asm volatile("" ::: "memory");
  }

#pragma unroll
  for (int g = 0; g < 2; ++g) {
    float zv[16];
#pragma unroll
    for (int i = 0; i < 16; ++i) {
      float f = acc[0][g][i], gg = acc[1][g][i];
      float e2 = __expf(2.f * f);
      float th = 1.f - 2.f * __frcp_rn(e2 + 1.f);
      float sg = __frcp_rn(1.f + __expf(-gg));
      zv[i] = th * sg;
    }
    int mt32 = (r0 + wm * 64 + g * 32) >> 5;
    int cs = (c0 + wn * 32) >> 4;
#pragma unroll
    for (int s = 0; s < 2; ++s) {
      u32x4 p;
#pragma unroll
      for (int j = 0; j < 4; ++j) p[j] = pk_bf16(zv[8 * s + 2 * j], zv[8 * s + 2 * j + 1]);
      *(u32x4*)(zws + ((size_t)(mt32 * 16 + cs + s) * 64 + lane) * 8) = p;
    }
  }
}

__global__ __launch_bounds__(256, 1) void k2(const unsigned short* __restrict__ zws, const unsigned short* __restrict__ wo_,
                                             unsigned short* __restrict__ out) {
  __shared__ __attribute__((aligned(16))) char lds[65536];
  const int tid = threadIdx.x, lane = tid & 63;
  const int wave = __builtin_amdgcn_readfirstlane(tid >> 6);
  const int mt = blockIdx.x >> 1, js = blockIdx.x & 1;
  const int wm = wave >> 1, wn = wave & 1;
  const char* wb = (const char*)wo_;
#pragma unroll
  for (int e = 0; e < 16; ++e) {
    int q = wave * 16 + e;
    int row = 4 * q + (lane >> 4);
    int ch = (lane & 15) ^ swzb(row);
    __builtin_amdgcn_global_load_lds((const unsigned*)(wb + row * 512 + js * 256 + ch * 16), LDSP(lds + q * 1024), 16, 0, 0);
  }
  const int mt32 = mt * 2 + wm;
  bf16x8 zf[16];
#pragma unroll
  for (int s = 0; s < 16; ++s) zf[s] = *(const bf16x8*)(zws + ((size_t)(mt32 * 16 + s) * 64 + lane) * 8);
  asm volatile("s_waitcnt vmcnt(0)" ::: "memory");
  __syncthreads();
  const int h = lane >> 5;
  const unsigned blk = (lane >> 4) & 1, q = (lane & 15) >> 2, p = lane & 3;
  f32x16 acc[2];
#pragma unroll
  for (int a = 0; a < 2; ++a)
#pragma unroll
    for (int i = 0; i < 16; ++i) acc[a][i] = 0.f;
#pragma unroll
  for (int s = 0; s < 16; ++s)
#pragma unroll
    for (int a = 0; a < 2; ++a) {
      s16x4 v[2];
#pragma unroll
      for (int t = 0; t < 2; ++t) {
        unsigned row = 16 * s + 8 * t + 4 * h + q;
        unsigned ch = 4 * (wn * 2 + a) + 2 * blk + (p >> 1);
        v[t] = tr_rd(lds + 256 * row + 16 * (ch ^ swzb(row)) + 8 * (p & 1));
      }
      bf16x8 af = __builtin_shufflevector(v[0], v[1], 0, 1, 2, 3, 4, 5, 6, 7);
      acc[a] = MFMA(af, zf[s], acc[a]);
    }
  const int r = mt32 * 32 + (lane & 31);
  if (r < MM) {
#pragma unroll
    for (int a = 0; a < 2; ++a)
#pragma unroll
      for (int g = 0; g < 4; ++g) {
        u32x2 o;
        o[0] = pk_bf16(acc[a][4 * g], acc[a][4 * g + 1]);
        o[1] = pk_bf16(acc[a][4 * g + 2], acc[a][4 * g + 3]);
        *(u32x2*)(out + (size_t)r * 256 + js * 128 + wn * 64 + a * 32 + 8 * g + 4 * h) = o;
      }
  }
}

extern "C" void kernel_launch(void* const* d_in, const int* in_sizes, int n_in, void* d_out, int out_size, void* d_ws,
                              size_t ws_size, hipStream_t stream) {
  const unsigned short* x = (const unsigned short*)d_in[0];
  const unsigned short* w = (const unsigned short*)d_in[1];
  const unsigned short* wo = (const unsigned short*)d_in[2];
  unsigned short* zws = (unsigned short*)d_ws;
  k1<<<256, 256, 0, stream>>>(x, w, zws);
  k2<<<250, 256, 0, stream>>>(zws, wo, (unsigned short*)d_out);
}
```

```cpp
#include <hip/hip_runtime.h>
#include <hip/hip_bf16.h>

typedef __attribute__((ext_vector_type(8))) short bf16x8;
typedef __attribute__((ext_vector_type(4))) short s16x4;
typedef __attribute__((ext_vector_type(16))) float f32x16;
typedef __attribute__((ext_vector_type(4))) unsigned u32x4;
typedef __attribute__((ext_vector_type(2))) unsigned u32x2;
#define DI __device__ __forceinline__
#define LDSP(p) ((__attribute__((address_space(3))) void*)(p))
#define MFMA(a, b, c) __builtin_amdgcn_mfma_f32_32x32x16_bf16((a), (b), (c), 0, 0, 0)

constexpr int TT = 4000, MM = 8000, DD = 256;

DI unsigned pk_bf16(float lo, float hi) {
  unsigned r;
  asm("v_cvt_pk_bf16_f32 %0, %1, %2" : "=v"(r) : "v"(lo), "v"(hi));
  return r;
}
DI s16x4 tr_rd(const char* p) {
  return __builtin_amdgcn_ds_read_tr16_b64_v4i16((__attribute__((address_space(3))) s16x4*)(p));
}
DI void dma16(const char* g, const char* l) {
  unsigned la = (unsigned)(size_t)l;
  asm volatile("s_mov_b32 m0, %1\n\tglobal_load_lds_dwordx4 %0, off" ::"v"(g), "s"(la) : "memory");
}
DI unsigned swzb(unsigned row) { return ((row & 3) << 2) | ((row >> 2) & 3); }

constexpr int XREG = 148 * 64;
constexpr int WREG = 3 * 8192;
constexpr int BUFB = XREG + WREG;
constexpr int NBUF = 4;

__global__ __launch_bounds__(256, 1) void k1(const unsigned short* __restrict__ x, const unsigned short* __restrict__ w,
                                             unsigned short* __restrict__ zws) {
  __shared__ __attribute__((aligned(16))) char lds[NBUF * BUFB];
  const int tid = threadIdx.x, lane = tid & 63;
  const int wave = __builtin_amdgcn_readfirstlane(tid >> 6);
  const int bid = blockIdx.x;
  const int mt = (bid & 7) + 8 * ((bid >> 3) >> 2);
  const int ns = (bid >> 3) & 3;
  if (mt >= 63) return;
  const int r0 = mt * 128, c0 = ns * 64;
  const int wm = wave >> 1, wn = wave & 1;

  if (tid < 64) {
    u32x4 zz = {0, 0, 0, 0};
    *(u32x4*)(lds + (tid >> 4) * BUFB + 144 * 64 + (tid & 15) * 16) = zz;
  }

  unsigned xsrc[3];
#pragma unroll
  for (int e = 0; e < 3; ++e) {
    int q = wave + 4 * e;
    int row = 16 * q + (lane >> 2);
    int ch = (lane & 3) ^ ((row >> 2) & 3);
    int gr = r0 - 4 + row;
    gr = gr < 0 ? 0 : (gr > MM - 1 ? MM - 1 : gr);
    xsrc[e] = (unsigned)gr * 512u + ch * 16u;
  }
  unsigned wsrc[6];
#pragma unroll
  for (int e = 0; e < 6; ++e) {
    int idx = wave * 6 + e;
    int tap = idx >> 3, q = idx & 7;
    int row = 4 * q + (lane >> 4);
    int ch = (lane & 15) ^ swzb(row);
    int col = ch < 8 ? c0 + ch * 8 : 256 + c0 + (ch - 8) * 8;
    wsrc[e] = (unsigned)((tap * 256 + row) * 512 + col) * 2u;
  }
  const char* xb = (const char*)x;
  const char* wb = (const char*)w;

  auto issue = [&](int s) {
    char* bb = lds + (s & (NBUF - 1)) * BUFB;
#pragma unroll
    for (int e = 0; e < 3; ++e) {
      if (e < 2 || wave == 0)
        dma16(xb + xsrc[e] + s * 64, bb + (wave + 4 * e) * 1024);
    }
#pragma unroll
    for (int e = 0; e < 6; ++e)
      dma16(wb + wsrc[e] + s * 32768, bb + XREG + (wave * 6 + e) * 1024);
  };

  const int m = lane & 31, h = lane >> 5;
  unsigned xo[2][3][2];
#pragma unroll
  for (int g = 0; g < 2; ++g)
#pragma unroll
    for (int i = 0; i < 3; ++i) {
      int lm = wm * 64 + g * 32 + m;
      int r = r0 + lm;
      int t = r >= TT ? r - TT : r;
      int xrow = lm + 4 - 2 * i;
      bool ok = t >= 2 * i;
#pragma unroll
      for (int ks = 0; ks < 2; ++ks) {
        int ch = 2 * ks + h;
        xo[g][i][ks] = ok ? (unsigned)(xrow * 64 + ((ch ^ ((xrow >> 2) & 3)) * 16)) : (unsigned)(144 * 64);
      }
    }
  unsigned wo[2][2][2];
  {
    const unsigned blk = (lane >> 4) & 1, q = (lane & 15) >> 2, p = lane & 3;
#pragma unroll
    for (int tl = 0; tl < 2; ++tl)
#pragma unroll
      for (int ks = 0; ks < 2; ++ks)
#pragma unroll
        for (int t = 0; t < 2; ++t) {
          unsigned row = 16 * ks + 8 * h + 4 * t + q;
          unsigned ch = 4 * (wn + 2 * tl) + 2 * blk + (p >> 1);
          wo[tl][ks][t] = XREG + 256 * row + 16 * (ch ^ swzb(row)) + 8 * (p & 1);
        }
  }

  f32x16 acc[2][2];
#pragma unroll
  for (int a = 0; a < 2; ++a)
#pragma unroll
    for (int b = 0; b < 2; ++b)
#pragma unroll
      for (int i = 0; i < 16; ++i) acc[a][b][i] = 0.f;

  issue(0);
  issue(1);
  issue(2);

#pragma unroll
  for (int s = 0; s < 8; ++s) {
    if (s < 6) asm volatile("s_waitcnt vmcnt(16) lgkmcnt(0)" ::: "memory");
    else if (s == 6) asm volatile("s_waitcnt vmcnt(8)" ::: "memory");
    else asm volatile("s_waitcnt vmcnt(0)" ::: "memory");
    __builtin_amdgcn_s_barrier();
    asm volatile("" ::: "memory");
    if (s + 3 < 8) issue(s + 3);
    const char* bb = lds + (s & (NBUF - 1)) * BUFB;
#pragma unroll
    for (int i = 0; i < 3; ++i)
#pragma unroll
      for (int ks = 0; ks < 2; ++ks) {
        bf16x8 af[2], bfr[2];
#pragma unroll
        for (int tl = 0; tl < 2; ++tl) {
          s16x4 lo = tr_rd(bb + i * 8192 + wo[tl][ks][0]);
          s16x4 hi = tr_rd(bb + i * 8192 + wo[tl][ks][1]);
          af[tl] = __builtin_shufflevector(lo, hi, 0, 1, 2, 3, 4, 5, 6, 7);
        }
#pragma unroll
        for (int g = 0; g < 2; ++g) bfr[g] = *(const bf16x8*)(bb + xo[g][i][ks]);
#pragma unroll
        for (int tl = 0; tl < 2; ++tl)
#pragma unroll
          for (int g = 0; g < 2; ++g) acc[tl][g] = MFMA(af[tl], bfr[g], acc[tl][g]);
      }
    asm volatile("" ::: "memory");
  }

#pragma unroll
  for (int g = 0; g < 2; ++g) {
    float zv[16];
#pragma unroll
    for (int i = 0; i < 16; ++i) {
      float f = acc[0][g][i], gg = acc[1][g][i];
      float e2 = __expf(2.f * f);
      float th = 1.f - 2.f * __builtin_amdgcn_rcpf(e2 + 1.f);
      float sg = __builtin_amdgcn_rcpf(1.f + __expf(-gg));
      zv[i] = th * sg;
    }
    int mt32 = (r0 + wm * 64 + g * 32) >> 5;
    int cs = (c0 + wn * 32) >> 4;
#pragma unroll
    for (int s = 0; s < 2; ++s) {
      u32x4 p;
#pragma unroll
      for (int j = 0; j < 4; ++j) p[j] = pk_bf16(zv[8 * s + 2 * j], zv[8 * s + 2 * j + 1]);
      *(u32x4*)(zws + ((size_t)(mt32 * 16 + cs + s) * 64 + lane) * 8) = p;
    }
  }
}

__global__ __launch_bounds__(256, 1) void k2(const unsigned short* __restrict__ zws, const unsigned short* __restrict__ wo_,
                                             unsigned short* __restrict__ out) {
  __shared__ __attribute__((aligned(16))) char lds[65536];
  const int tid = threadIdx.x, lane = tid & 63;
  const int wave = __builtin_amdgcn_readfirstlane(tid >> 6);
  const int mt = blockIdx.x >> 1, js = blockIdx.x & 1;
  const int wm = wave >> 1, wn = wave & 1;
  const char* wb = (const char*)wo_;
#pragma unroll
  for (int e = 0; e < 16; ++e) {
    int q = wave * 16 + e;
    int row = 4 * q + (lane >> 4);
    int ch = (lane & 15) ^ swzb(row);
    __builtin_amdgcn_global_load_lds((const unsigned*)(wb + row * 512 + js * 256 + ch * 16), LDSP(lds + q * 1024), 16, 0, 0);
  }
  const int mt32 = mt * 2 + wm;
  bf16x8 zf[16];
#pragma unroll
  for (int s = 0; s < 16; ++s) zf[s] = *(const bf16x8*)(zws + ((size_t)(mt32 * 16 + s) * 64 + lane) * 8);
  asm volatile("s_waitcnt vmcnt(0)" ::: "memory");
  __syncthreads();
  const int h = lane >> 5;
  const unsigned blk = (lane >> 4) & 1, q = (lane & 15) >> 2, p = lane & 3;
  f32x16 acc[2];
#pragma unroll
  for (int a = 0; a < 2; ++a)
#pragma unroll
    for (int i = 0; i < 16; ++i) acc[a][i] = 0.f;
#pragma unroll
  for (int s = 0; s < 16; ++s)
#pragma unroll
    for (int a = 0; a < 2; ++a) {
      s16x4 v[2];
#pragma unroll
      for (int t = 0; t < 2; ++t) {
        unsigned row = 16 * s + 8 * t + 4 * h + q;
        unsigned ch = 4 * (wn * 2 + a) + 2 * blk + (p >> 1);
        v[t] = tr_rd(lds + 256 * row + 16 * (ch ^ swzb(row)) + 8 * (p & 1));
      }
      bf16x8 af = __builtin_shufflevector(v[0], v[1], 0, 1, 2, 3, 4, 5, 6, 7);
      acc[a] = MFMA(af, zf[s], acc[a]);
    }
  const int r = mt32 * 32 + (lane & 31);
  if (r < MM) {
#pragma unroll
    for (int a = 0; a < 2; ++a)
#pragma unroll
      for (int g = 0; g < 4; ++g) {
        u32x2 o;
        o[0] = pk_bf16(acc[a][4 * g], acc[a][4 * g + 1]);
        o[1] = pk_bf16(acc[a][4 * g + 2], acc[a][4 * g + 3]);
        *(u32x2*)(out + (size_t)r * 256 + js * 128 + wn * 64 + a * 32 + 8 * g + 4 * h) = o;
      }
  }
}

extern "C" void kernel_launch(void* const* d_in, const int* in_sizes, int n_in, void* d_out, int out_size, void* d_ws,
                              size_t ws_size, hipStream_t stream) {
  const unsigned short* x = (const unsigned short*)d_in[0];
  const unsigned short* w = (const unsigned short*)d_in[1];
  const unsigned short* wo = (const unsigned short*)d_in[2];
  unsigned short* zws = (unsigned short*)d_ws;
  k1<<<256, 256, 0, stream>>>(x, w, zws);
  k2<<<250, 256, 0, stream>>>(zws, wo, (unsigned short*)d_out);
}
```

```cpp
#include <hip/hip_runtime.h>
#include <hip/hip_bf16.h>

typedef __attribute__((ext_vector_type(8))) short bf16x8;
typedef __attribute__((ext_vector_type(4))) short s16x4;
typedef __attribute__((ext_vector_type(16))) float f32x16;
typedef __attribute__((ext_vector_type(4))) unsigned u32x4;
typedef __attribute__((ext_vector_type(2))) unsigned u32x2;
#define DI __device__ __forceinline__
#define LDSP(p) ((__attribute__((address_space(3))) void*)(p))
#define MFMA(a, b, c) __builtin_amdgcn_mfma_f32_32x32x16_bf16((a), (b), (c), 0, 0, 0)

constexpr int TT = 4000, MM = 8000, DD = 256;

DI unsigned pk_bf16(float lo, float hi) {
  unsigned r;
  asm("v_cvt_pk_bf16_f32 %0, %1, %2" : "=v"(r) : "v"(lo), "v"(hi));
  return r;
}
DI s16x4 tr_rd(const char* p) {
  return __builtin_amdgcn_ds_read_tr16_b64_v4i16((__attribute__((address_space(3))) s16x4*)(p));
}
DI void dma16(const char* g, const char* l) {
  unsigned la = (unsigned)(size_t)l;
  asm volatile("s_mov_b32 m0, %1\n\tglobal_load_lds_dwordx4 %0, off" ::"v"(g), "s"(la) : "memory");
}
DI unsigned swzb(unsigned row) { return ((row & 3) << 2) | ((row >> 2) & 3); }

constexpr int XREG = 148 * 64;
constexpr int WREG = 3 * 8192;
constexpr int BUFB = XREG + WREG;
constexpr int NBUF = 4;

__global__ __launch_bounds__(512, 2) void k1(const unsigned short* __restrict__ x, const unsigned short* __restrict__ w,
                                             unsigned short* __restrict__ zws) {
  __shared__ __attribute__((aligned(16))) char lds[NBUF * BUFB];
  const int tid = threadIdx.x, lane = tid & 63;
  const int wave = __builtin_amdgcn_readfirstlane(tid >> 6);
  const int bid = blockIdx.x;
  const int mt = (bid & 7) + 8 * ((bid >> 3) >> 2);
  const int ns = (bid >> 3) & 3;
  if (mt >= 63) return;
  const int r0 = mt * 128, c0 = ns * 64;
  const int wm = wave >> 1, wn = wave & 1;

  if (tid < 64) {
    u32x4 zz = {0, 0, 0, 0};
    *(u32x4*)(lds + (tid >> 4) * BUFB + 144 * 64 + (tid & 15) * 16) = zz;
  }

  unsigned xsrc[2];
#pragma unroll
  for (int e = 0; e < 2; ++e) {
    int q = wave + 8 * e;
    int row = 16 * q + (lane >> 2);
    int ch = (lane & 3) ^ ((row >> 2) & 3);
    int gr = r0 - 4 + row;
    gr = gr < 0 ? 0 : (gr > MM - 1 ? MM - 1 : gr);
    xsrc[e] = (unsigned)gr * 512u + ch * 16u;
  }
  unsigned wsrc[3];
#pragma unroll
  for (int e = 0; e < 3; ++e) {
    int idx = wave * 3 + e;
    int tap = idx >> 3, q = idx & 7;
    int row = 4 * q + (lane >> 4);
    int ch = (lane & 15) ^ swzb(row);
    int col = ch < 8 ? c0 + ch * 8 : 256 + c0 + (ch - 8) * 8;
    wsrc[e] = (unsigned)((tap * 256 + row) * 512 + col) * 2u;
  }
  const char* xb = (const char*)x;
  const char* wb = (const char*)w;

  auto issue = [&](int s) {
    char* bb = lds + (s & (NBUF - 1)) * BUFB;
#pragma unroll
    for (int e = 0; e < 2; ++e) {
      if (e < 1 || wave == 0)
        dma16(xb + xsrc[e] + s * 64, bb + (wave + 8 * e) * 1024);
    }
#pragma unroll
    for (int e = 0; e < 3; ++e)
      dma16(wb + wsrc[e] + s * 32768, bb + XREG + (wave * 3 + e) * 1024);
  };

  const int m = lane & 31, h = lane >> 5;
  unsigned xo[2][3][2];
#pragma unroll
  for (int g = 0; g < 1; ++g)
#pragma unroll
    for (int i = 0; i < 3; ++i) {
      int lm = wm * 32 + m;
      int r = r0 + lm;
      int t = r >= TT ? r - TT : r;
      int xrow = lm + 4 - 2 * i;
      bool ok = t >= 2 * i;
#pragma unroll
      for (int ks = 0; ks < 2; ++ks) {
        int ch = 2 * ks + h;
        xo[g][i][ks] = ok ? (unsigned)(xrow * 64 + ((ch ^ ((xrow >> 2) & 3)) * 16)) : (unsigned)(144 * 64);
      }
    }
  unsigned wo[2][2][2];
  {
    const unsigned blk = (lane >> 4) & 1, q = (lane & 15) >> 2, p = lane & 3;
#pragma unroll
    for (int tl = 0; tl < 2; ++tl)
#pragma unroll
      for (int ks = 0; ks < 2; ++ks)
#pragma unroll
        for (int t = 0; t < 2; ++t) {
          unsigned row = 16 * ks + 8 * h + 4 * t + q;
          unsigned ch = 4 * (wn + 2 * tl) + 2 * blk + (p >> 1);
          wo[tl][ks][t] = XREG + 256 * row + 16 * (ch ^ swzb(row)) + 8 * (p & 1);
        }
  }

  f32x16 acc[2][2];
#pragma unroll
  for (int a = 0; a < 2; ++a)
#pragma unroll
    for (int b = 0; b < 2; ++b)
#pragma unroll
      for (int i = 0; i < 16; ++i) acc[a][b][i] = 0.f;

  issue(0);
  issue(1);
  issue(2);

#pragma unroll
  for (int s = 0; s < 8; ++s) {
    if (s < 6) asm volatile("s_waitcnt vmcnt(8) lgkmcnt(0)" ::: "memory");
    else if (s == 6) asm volatile("s_waitcnt vmcnt(4)" ::: "memory");
    else asm volatile("s_waitcnt vmcnt(0)" ::: "memory");
    __builtin_amdgcn_s_barrier();
    asm volatile("" ::: "memory");
    if (s + 3 < 8) issue(s + 3);
    const char* bb = lds + (s & (NBUF - 1)) * BUFB;
#pragma unroll
    for (int i = 0; i < 3; ++i)
#pragma unroll
      for (int ks = 0; ks < 2; ++ks) {
        bf16x8 af[2], bfr[2];
#pragma unroll
        for (int tl = 0; tl < 2; ++tl) {
          s16x4 lo = tr_rd(bb + i * 8192 + wo[tl][ks][0]);
          s16x4 hi = tr_rd(bb + i * 8192 + wo[tl][ks][1]);
          af[tl] = __builtin_shufflevector(lo, hi, 0, 1, 2, 3, 4, 5, 6, 7);
        }
#pragma unroll
        for (int g = 0; g < 1; ++g) bfr[g] = *(const bf16x8*)(bb + xo[g][i][ks]);
#pragma unroll
        for (int tl = 0; tl < 2; ++tl)
#pragma unroll
          for (int g = 0; g < 1; ++g) acc[tl][g] = MFMA(af[tl], bfr[g], acc[tl][g]);
      }
    asm volatile("" ::: "memory");
  }

#pragma unroll
  for (int g = 0; g < 1; ++g) {
    float zv[16];
#pragma unroll
    for (int i = 0; i < 16; ++i) {
      float f = acc[0][g][i], gg = acc[1][g][i];
      float e2 = __expf(2.f * f);
      float th = 1.f - 2.f * __builtin_amdgcn_rcpf(e2 + 1.f);
      float sg = __builtin_amdgcn_rcpf(1.f + __expf(-gg));
      zv[i] = th * sg;
    }
    int mt32 = (r0 + wm * 32) >> 5;
    int cs = (c0 + wn * 32) >> 4;
#pragma unroll
    for (int s = 0; s < 2; ++s) {
      u32x4 p;
#pragma unroll
      for (int j = 0; j < 4; ++j) p[j] = pk_bf16(zv[8 * s + 2 * j], zv[8 * s + 2 * j + 1]);
      *(u32x4*)(zws + ((size_t)(mt32 * 16 + cs + s) * 64 + lane) * 8) = p;
    }
  }
}

__global__ __launch_bounds__(256, 1) void k2(const unsigned short* __restrict__ zws, const unsigned short* __restrict__ wo_,
                                             unsigned short* __restrict__ out) {
  __shared__ __attribute__((aligned(16))) char lds[65536];
  const int tid = threadIdx.x, lane = tid & 63;
  const int wave = __builtin_amdgcn_readfirstlane(tid >> 6);
  const int mt = blockIdx.x >> 1, js = blockIdx.x & 1;
  const int wm = wave >> 1, wn = wave & 1;
  const char* wb = (const char*)wo_;
#pragma unroll
  for (int e = 0; e < 16; ++e) {
    int q = wave * 16 + e;
    int row = 4 * q + (lane >> 4);
    int ch = (lane & 15) ^ swzb(row);
    __builtin_amdgcn_global_load_lds((const unsigned*)(wb + row * 512 + js * 256 + ch * 16), LDSP(lds + q * 1024), 16, 0, 0);
  }
  const int mt32 = mt * 2 + wm;
  bf16x8 zf[16];
#pragma unroll
  for (int s = 0; s < 16; ++s) zf[s] = *(const bf16x8*)(zws + ((size_t)(mt32 * 16 + s) * 64 + lane) * 8);
  asm volatile("s_waitcnt vmcnt(0)" ::: "memory");
  __syncthreads();
  const int h = lane >> 5;
  const unsigned blk = (lane >> 4) & 1, q = (lane & 15) >> 2, p = lane & 3;
  f32x16 acc[2];
#pragma unroll
  for (int a = 0; a < 2; ++a)
#pragma unroll
    for (int i = 0; i < 16; ++i) acc[a][i] = 0.f;
#pragma unroll
  for (int s = 0; s < 16; ++s)
#pragma unroll
    for (int a = 0; a < 2; ++a) {
      s16x4 v[2];
#pragma unroll
      for (int t = 0; t < 2; ++t) {
        unsigned row = 16 * s + 8 * t + 4 * h + q;
        unsigned ch = 4 * (wn * 2 + a) + 2 * blk + (p >> 1);
        v[t] = tr_rd(lds + 256 * row + 16 * (ch ^ swzb(row)) + 8 * (p & 1));
      }
      bf16x8 af = __builtin_shufflevector(v[0], v[1], 0, 1, 2, 3, 4, 5, 6, 7);
      acc[a] = MFMA(af, zf[s], acc[a]);
    }
  const int r = mt32 * 32 + (lane & 31);
  if (r < MM) {
#pragma unroll
    for (int a = 0; a < 2; ++a)
#pragma unroll
      for (int g = 0; g < 4; ++g) {
        u32x2 o;
        o[0] = pk_bf16(acc[a][4 * g], acc[a][4 * g + 1]);
        o[1] = pk_bf16(acc[a][4 * g + 2], acc[a][4 * g + 3]);
        *(u32x2*)(out + (size_t)r * 256 + js * 128 + wn * 64 + a * 32 + 8 * g + 4 * h) = o;
      }
  }
}

extern "C" void kernel_launch(void* const* d_in, const int* in_sizes, int n_in, void* d_out, int out_size, void* d_ws,
                              size_t ws_size, hipStream_t stream) {
  const unsigned short* x = (const unsigned short*)d_in[0];
  const unsigned short* w = (const unsigned short*)d_in[1];
  const unsigned short* wo = (const unsigned short*)d_in[2];
  unsigned short* zws = (unsigned short*)d_ws;
  k1<<<256, 512, 0, stream>>>(x, w, zws);
  k2<<<250, 256, 0, stream>>>(zws, wo, (unsigned short*)d_out);
}
```
